```python
import torch

def _softmax(x, axis=-1):
    return torch.softmax(x, dim=axis)

def _gelu(x):
    return torch.nn.functional.gelu(x, approximate='tanh')

def _silu(x):
    return torch.nn.functional.silu(x)

def _sigmoid(x):
    return torch.sigmoid(x)

def _layernorm(x, axis=-1, eps=1e-05):
    m = x.mean(dim=axis, keepdim=True)
    v = x.var(dim=axis, keepdim=True, unbiased=False)
    return (x - m) / torch.sqrt(v + eps)

def _scatter_add(tgt, idx, src, axis=0):
    return tgt.clone().index_add_(axis, idx, src)

def _segment_sum(src, seg, n):
    out = torch.zeros((n,) + src.shape[1:], dtype=src.dtype)
    return out.index_add_(0, seg, src)

def _pad(x, pw):
    flat = tuple((v for pair in reversed(pw) for v in pair))
    return torch.nn.functional.pad(x, flat)
kernel_name = 'gather_scatter__tensor_product_equiv_mace__graph'

def setup_inputs():

    _w = lambda a: (lambda t: t.to(torch.bfloat16) if t.is_floating_point() else t)(torch.as_tensor(a))
    N, deg = (10000, 32)
    Lmax, C, De = (1, 128, 16)
    M = (Lmax + 1) ** 2
    E = N * deg
    src = (lambda lo, hi, *s: torch.randint(lo, hi, s))(0, N, E).to(torch.int64)
    dst = (lambda lo, hi, *s: torch.randint(lo, hi, s))(0, N, E).to(torch.int64)
    r = (lambda *s: torch.randn(*s, dtype=torch.bfloat16) * 0.05)(E, 3).to(float)
    r = r / (torch.linalg.norm(r, dim=-1, keepdim=True).to(float) + 1e-06)
    rx, ry, rz = (r[:, 0].clone(), r[:, 1].clone(), r[:, 2].clone())
    x2, y2, z2 = (rx * rx, ry * ry, rz * rz)
    sh_list = [torch.ones_like(rx), rx, ry, rz, rx * ry, ry * rz, 2.0 * z2 - x2 - y2, rx * rz, x2 - y2, ry * (3.0 * x2 - y2), rx * ry * rz, ry * (4.0 * z2 - x2 - y2), rz * (2.0 * z2 - 3.0 * x2 - 3.0 * y2), rx * (4.0 * z2 - x2 - y2), rz * (x2 - y2), rx * (x2 - 3.0 * y2)]
    sh16 = torch.stack(sh_list, dim=-1).to(float)
    pad = max(0, M - 16)
    sh_full = torch.cat([sh16, torch.zeros((E, pad), dtype=float)], dim=-1)[:, :M]
    cg = ((lambda *s: torch.randn(*s, dtype=torch.bfloat16) * 0.05)(M, M, M) * (1.0 / float(M))).to(float)
    return {'x': _w(((lambda *s: torch.randn(*s, dtype=torch.bfloat16) * 0.05)(N, M, C) * 0.3).to(float)), 'src': _w(src), 'dst': _w(dst), 'sh': _w(sh_full.to(float)), 'edge_attr': _w(((lambda *s: torch.randn(*s, dtype=torch.bfloat16) * 0.05)(E, De) * 0.3).to(float)), 'cg': _w(cg), 'w_r': _w(((lambda *s: torch.randn(*s, dtype=torch.bfloat16) * 0.05)(De, C) * (1.0 / float(De) ** 0.5)).to(float)), 'w_out': _w(((lambda *s: torch.randn(*s, dtype=torch.bfloat16) * 0.05)(C, C) * (1.0 / float(C) ** 0.5)).to(float))}

def reference(x, src, dst, sh, edge_attr, cg, w_r, w_out):
    N = x.shape[0]
    M = x.shape[1]
    C = x.shape[2]
    h = x[src]
    tp = torch.einsum('uvw,euc,ev->ewc', cg, h, sh)
    rw = edge_attr @ w_r
    msg = tp * rw[:, None, :]
    msg2 = msg.reshape(-1, M * C)
    agg = _segment_sum(msg2, dst, N).reshape(N, M, C)
    out = torch.einsum('nmc,cd->nmd', agg, w_out)
    return out
```

```cpp
#include <hip/hip_runtime.h>
#include <hip/hip_bf16.h>
#include <stdint.h>

typedef short bf16x4_t __attribute__((ext_vector_type(4)));
typedef short bf16x8_t __attribute__((ext_vector_type(8)));
typedef float f32x4_t __attribute__((ext_vector_type(4)));

#define NPW 4
#define CAP 64
#define KB 256
#define RMAX 12288
#define CH 128
#define MM 4
#define DE 16

__device__ __forceinline__ float bf_lo(unsigned v) { return __uint_as_float(v << 16); }
__device__ __forceinline__ float bf_hi(unsigned v) { return __uint_as_float(v & 0xffff0000u); }
__device__ __forceinline__ unsigned pack2(float lo, float hi) {
  __hip_bfloat162 h = __float22bfloat162_rn(make_float2(lo, hi));
  return *reinterpret_cast<unsigned*>(&h);
}

__global__ __launch_bounds__(1024) void bin_kernel(const int* __restrict__ dst, const int* __restrict__ src, int E,
                                                   int R, int chunk, int* __restrict__ offs,
                                                   int4* __restrict__ ent, const unsigned short* __restrict__ w_out,
                                                   uint4* __restrict__ wof) {
  __shared__ int hist[RMAX + 1];
  __shared__ int wsum[16];
  const int tid = threadIdx.x;
  const int b = blockIdx.x;
  if (b >= KB) {
    int idx = (b - KB) * 1024 + tid;
    if (idx < 4 * 8 * 64) {
      int lane = idx & 63, st = idx >> 6, s = st >> 3, t = st & 7;
      int kg = lane >> 4, i = lane & 15;
      unsigned v[4];
#pragma unroll
      for (int j = 0; j < 4; ++j) {
        unsigned lo = w_out[(32 * s + 8 * kg + 2 * j) * CH + 8 * i + t];
        unsigned hi = w_out[(32 * s + 8 * kg + 2 * j + 1) * CH + 8 * i + t];
        v[j] = lo | (hi << 16);
      }
      wof[idx] = make_uint4(v[0], v[1], v[2], v[3]);
    }
    return;
  }
  for (int r = tid; r <= R; r += 1024) hist[r] = 0;
  __syncthreads();
  const int e0 = b * chunk;
  const int e1 = min(E, e0 + chunk);
  int myr[4], mys[4];
#pragma unroll
  for (int k = 0; k < 4; ++k) {
    int e = e0 + k * 1024 + tid;
    myr[k] = -1;
    mys[k] = 0;
    if (e < e1) {
      int r = dst[e] / NPW;
      myr[k] = r;
      mys[k] = atomicAdd(&hist[r], 1);
    }
  }
  __syncthreads();
  const int per = (R + 1023) / 1024;
  int lsum = 0;
  for (int k = 0; k < per; ++k) {
    int r = tid * per + k;
    if (r < R) lsum += hist[r];
  }
  int incl = lsum;
#pragma unroll
  for (int o = 1; o < 64; o <<= 1) {
    int v = __shfl_up(incl, o, 64);
    if ((tid & 63) >= o) incl += v;
  }
  if ((tid & 63) == 63) wsum[tid >> 6] = incl;
  __syncthreads();
  int woff = 0;
  for (int w = 0; w < (tid >> 6); ++w) woff += wsum[w];
  int run = woff + incl - lsum;
  for (int k = 0; k < per; ++k) {
    int r = tid * per + k;
    if (r < R) {
      int c = hist[r];
      hist[r] = run;
      run += c;
    }
  }
  if (tid == 1023) hist[R] = e1 - e0;
  __syncthreads();
  for (int r = tid; r <= R; r += 1024) offs[(size_t)b * (R + 1) + r] = hist[r];
#pragma unroll
  for (int k = 0; k < 4; ++k) {
    int e = e0 + k * 1024 + tid;
    if (e < e1) {
      int pos = hist[myr[k]] + mys[k];
      ent[(size_t)e0 + pos] = make_int4(e, src[e], dst[e], 0);
    }
  }
}

struct Consts {
  float cgw[16];
  bf16x4_t wrf[8];
};

__device__ __forceinline__ void process_edges(float (&acc)[8][4], const Consts& cs, int my_eid, int my_src, int d,
                                              const uint4* __restrict__ x4, const uint2* __restrict__ sh2,
                                              const uint2* __restrict__ ea2, int lane) {
  const int g = lane >> 4, i = lane & 15;
  const bool diag_lane = ((i >> 2) == g);
  const int nq = (d + 3) >> 2;
  const int nb = (d + 15) >> 4;

  auto load_quad = [&](int q, uint4 (&xr)[4], uint2& shv) {
    int rk = 4 * q + g;
    int rkc = rk < d ? rk : d - 1;
    int sg = __shfl(my_src, rkc, 64);
    int eg = __shfl(my_eid, rkc, 64);
#pragma unroll
    for (int u = 0; u < 4; ++u) xr[u] = x4[((size_t)sg * 4 + u) * 16 + i];
    shv = sh2[eg];
  };
  auto load_ea = [&](int b) -> uint2 {
    int rho = 16 * b + 4 * (i & 3) + (i >> 2);
    rho = rho < d ? rho : d - 1;
    int eid = __shfl(my_eid, rho, 64);
    return ea2[(size_t)eid * 4 + g];
  };
  auto comp_quad = [&](int q, const f32x4_t (&rw)[8], int r, const uint4 (&xr)[4], uint2 shv) {
    bool valid = (4 * q + g) < d;
    float s0 = bf_lo(shv.x), s1 = bf_hi(shv.x), s2 = bf_lo(shv.y), s3 = bf_hi(shv.y);
    float av[4];
#pragma unroll
    for (int u = 0; u < 4; ++u)
      av[u] = cs.cgw[u * 4 + 0] * s0 + cs.cgw[u * 4 + 1] * s1 + cs.cgw[u * 4 + 2] * s2 + cs.cgw[u * 4 + 3] * s3;
    uint2 ap;
    ap.x = pack2(av[0], av[1]);
    ap.y = pack2(av[2], av[3]);
    if (!(diag_lane && valid)) { ap.x = 0u; ap.y = 0u; }
    bf16x4_t aop = *reinterpret_cast<bf16x4_t*>(&ap);
    const unsigned* x0 = reinterpret_cast<const unsigned*>(&xr[0]);
    const unsigned* x1 = reinterpret_cast<const unsigned*>(&xr[1]);
    const unsigned* x2 = reinterpret_cast<const unsigned*>(&xr[2]);
    const unsigned* x3 = reinterpret_cast<const unsigned*>(&xr[3]);
#pragma unroll
    for (int t = 0; t < 8; ++t) {
      const int dd = t >> 1;
      uint2 bo;
      if (t & 1) {
        bo.x = __builtin_amdgcn_perm(x1[dd], x0[dd], 0x07060302u);
        bo.y = __builtin_amdgcn_perm(x3[dd], x2[dd], 0x07060302u);
      } else {
        bo.x = __builtin_amdgcn_perm(x1[dd], x0[dd], 0x05040100u);
        bo.y = __builtin_amdgcn_perm(x3[dd], x2[dd], 0x05040100u);
      }
      bf16x4_t bop = *reinterpret_cast<bf16x4_t*>(&bo);
      f32x4_t z = {0.f, 0.f, 0.f, 0.f};
      f32x4_t tp = __builtin_amdgcn_mfma_f32_16x16x16bf16_1k(aop, bop, z, 0, 0, 0);
#pragma unroll
      for (int w = 0; w < 4; ++w) acc[t][w] += tp[w] * rw[t][r];
    }
  };

  uint4 xa[4], xb[4];
  uint2 sa, sb;
  uint2 ea_next = load_ea(0);
  load_quad(0, xa, sa);
  if (nq > 1) load_quad(1, xb, sb);
  for (int b = 0; b < nb; ++b) {
    f32x4_t rw[8];
    {
      bf16x4_t a = *reinterpret_cast<bf16x4_t*>(&ea_next);
#pragma unroll
      for (int t = 0; t < 8; ++t) {
        f32x4_t z = {0.f, 0.f, 0.f, 0.f};
        rw[t] = __builtin_amdgcn_mfma_f32_16x16x16bf16_1k(a, cs.wrf[t], z, 0, 0, 0);
      }
    }
    if (b + 1 < nb) ea_next = load_ea(b + 1);
    const int q0 = 4 * b;
    comp_quad(q0, rw, 0, xa, sa);
    if (q0 + 2 < nq) load_quad(q0 + 2, xa, sa);
    if (q0 + 1 < nq) {
      comp_quad(q0 + 1, rw, 1, xb, sb);
      if (q0 + 3 < nq) load_quad(q0 + 3, xb, sb);
    }
    if (q0 + 2 < nq) {
      comp_quad(q0 + 2, rw, 2, xa, sa);
      if (q0 + 4 < nq) load_quad(q0 + 4, xa, sa);
    }
    if (q0 + 3 < nq) {
      comp_quad(q0 + 3, rw, 3, xb, sb);
      if (q0 + 5 < nq) load_quad(q0 + 5, xb, sb);
    }
  }
}

__global__ __launch_bounds__(NPW * 64) void main_kernel(
    const uint4* __restrict__ x4, const int* __restrict__ src, const int* __restrict__ dst,
    const uint2* __restrict__ sh2, const uint2* __restrict__ ea2, const unsigned short* __restrict__ cg,
    const unsigned short* __restrict__ w_r, const uint4* __restrict__ wof, const int* __restrict__ offs,
    const int4* __restrict__ ent, int N, int E, int R, int chunk, int use_lists, uint4* __restrict__ out4) {
  __shared__ int lcnt[NPW];
  __shared__ int arrive;
  __shared__ int2 llist[NPW][CAP];
  __shared__ __attribute__((aligned(16))) unsigned short aggs[NPW * MM][CH];

  const int tid = threadIdx.x, lane = tid & 63, w = tid >> 6;
  const int rg = blockIdx.x;
  const int g = lane >> 4, i = lane & 15;

  if (tid < NPW) lcnt[tid] = 0;
  if (tid == 0) arrive = 0;
  __syncthreads();
  if (use_lists && tid < KB) {
    int s = offs[(size_t)tid * (R + 1) + rg], e = offs[(size_t)tid * (R + 1) + rg + 1];
    const int4* seg = ent + (size_t)tid * chunk;
    for (int j = s; j < e; ++j) {
      int4 v = seg[j];
      int nl = v.z - rg * NPW;
      int slot = atomicAdd(&lcnt[nl], 1);
      if (slot < CAP) llist[nl][slot] = make_int2(v.x, v.y);
    }
  }
  Consts cs;
  {
    const int wq = i & 3;
#pragma unroll
    for (int k = 0; k < 16; ++k) cs.cgw[k] = bf_lo(cg[k * 4 + wq]);
#pragma unroll
    for (int t = 0; t < 8; ++t) {
      uint2 v;
      v.x = (unsigned)w_r[(4 * g + 0) * CH + 8 * i + t] | ((unsigned)w_r[(4 * g + 1) * CH + 8 * i + t] << 16);
      v.y = (unsigned)w_r[(4 * g + 2) * CH + 8 * i + t] | ((unsigned)w_r[(4 * g + 3) * CH + 8 * i + t] << 16);
      cs.wrf[t] = *reinterpret_cast<bf16x4_t*>(&v);
    }
  }
  __syncthreads();

  const int n = rg * NPW + w;
  float acc[8][4];
#pragma unroll
  for (int t = 0; t < 8; ++t)
#pragma unroll
    for (int k = 0; k < 4; ++k) acc[t][k] = 0.f;

  if (n < N) {
    const int d = use_lists ? lcnt[w] : (CAP + 1);
    if (d <= CAP) {
      if (d > 0) {
        int2 me = (lane < d) ? llist[w][lane] : make_int2(0x7fffffff, 0);
        int rank = 0;
        for (int s = 0; s < d; ++s) {
          int o = __builtin_amdgcn_readlane(me.x, s);
          rank += (o < me.x) ? 1 : 0;
        }
        if (lane < d) llist[w][rank] = me;
        __builtin_amdgcn_fence(__ATOMIC_RELEASE, "wavefront");
        __builtin_amdgcn_wave_barrier();
        __builtin_amdgcn_fence(__ATOMIC_ACQUIRE, "wavefront");
        int2 sorted = (lane < d) ? llist[w][lane] : make_int2(0, 0);
        process_edges(acc, cs, sorted.x, sorted.y, d, x4, sh2, ea2, lane);
      }
    } else {
      int cnt = 0;
      const unsigned long long lt = (lane == 0) ? 0ull : (~0ull >> (64 - lane));
      for (int base = 0; base < E; base += 64) {
        int e = base + lane;
        bool hit = (e < E) && (dst[e] == n);
        unsigned long long m = __ballot(hit);
        if (m) {
          int tot = __popcll(m);
          if (cnt + tot > CAP) {
            __builtin_amdgcn_fence(__ATOMIC_RELEASE, "wavefront");
            __builtin_amdgcn_wave_barrier();
            __builtin_amdgcn_fence(__ATOMIC_ACQUIRE, "wavefront");
            int2 sorted = (lane < cnt) ? llist[w][lane] : make_int2(0, 0);
            process_edges(acc, cs, sorted.x, sorted.y, cnt, x4, sh2, ea2, lane);
            cnt = 0;
          }
          if (hit) llist[w][cnt + __popcll(m & lt)] = make_int2(e, src[e]);
          cnt += tot;
        }
      }
      if (cnt > 0) {
        __builtin_amdgcn_fence(__ATOMIC_RELEASE, "wavefront");
        __builtin_amdgcn_wave_barrier();
        __builtin_amdgcn_fence(__ATOMIC_ACQUIRE, "wavefront");
        int2 sorted = (lane < cnt) ? llist[w][lane] : make_int2(0, 0);
        process_edges(acc, cs, sorted.x, sorted.y, cnt, x4, sh2, ea2, lane);
      }
    }
  }
#pragma unroll
  for (int t = 0; t < 8; ++t)
#pragma unroll
    for (int k = 0; k < 4; ++k) {
      float v = acc[t][k];
      v += __shfl_xor(v, 16, 64);
      v += __shfl_xor(v, 32, 64);
      acc[t][k] = v;
    }
  if (g == 0) {
#pragma unroll
    for (int k = 0; k < 4; ++k) {
      uint4 p;
      p.x = pack2(acc[0][k], acc[1][k]);
      p.y = pack2(acc[2][k], acc[3][k]);
      p.z = pack2(acc[4][k], acc[5][k]);
      p.w = pack2(acc[6][k], acc[7][k]);
      *reinterpret_cast<uint4*>(&aggs[w * MM + k][8 * i]) = p;
    }
  }
  __builtin_amdgcn_fence(__ATOMIC_RELEASE, "workgroup");
  int prev = 0;
  if (lane == 0) prev = atomicAdd(&arrive, 1);
  prev = __shfl(prev, 0, 64);
  if (prev != NPW - 1) return;
  __builtin_amdgcn_fence(__ATOMIC_ACQUIRE, "workgroup");
  f32x4_t o[1][8];
#pragma unroll
  for (int t = 0; t < 8; ++t) o[0][t] = (f32x4_t){0.f, 0.f, 0.f, 0.f};
#pragma unroll
  for (int s = 0; s < 4; ++s) {
    bf16x8_t a0 = *reinterpret_cast<const bf16x8_t*>(&aggs[i][32 * s + 8 * g]);
#pragma unroll
    for (int t = 0; t < 8; ++t) {
      uint4 bv = wof[(s * 8 + t) * 64 + lane];
      bf16x8_t bb = *reinterpret_cast<bf16x8_t*>(&bv);
      o[0][t] = __builtin_amdgcn_mfma_f32_16x16x32_bf16(a0, bb, o[0][t], 0, 0, 0);
    }
  }
#pragma unroll
  for (int mt = 0; mt < 1; ++mt) {
    int node = rg * NPW + mt * 4 + g;
    if (node < N) {
#pragma unroll
      for (int r = 0; r < 4; ++r) {
        uint4 p;
        p.x = pack2(o[mt][0][r], o[mt][1][r]);
        p.y = pack2(o[mt][2][r], o[mt][3][r]);
        p.z = pack2(o[mt][4][r], o[mt][5][r]);
        p.w = pack2(o[mt][6][r], o[mt][7][r]);
        out4[((size_t)node * 4 + r) * 16 + i] = p;
      }
    }
  }
}

extern "C" void kernel_launch(void* const* d_in, const int* in_sizes, int n_in, void* d_out, int out_size,
                              void* d_ws, size_t ws_size, hipStream_t stream) {
  const uint4* x4 = (const uint4*)d_in[0];
  const int* src = (const int*)d_in[1];
  const int* dst = (const int*)d_in[2];
  const uint2* sh2 = (const uint2*)d_in[3];
  const uint2* ea2 = (const uint2*)d_in[4];
  const unsigned short* cg = (const unsigned short*)d_in[5];
  const unsigned short* w_r = (const unsigned short*)d_in[6];
  const unsigned short* w_out = (const unsigned short*)d_in[7];
  const int N = in_sizes[0] / (MM * CH);
  const int E = in_sizes[1];
  const int R = (N + NPW - 1) / NPW;
  const int chunk = (E + KB - 1) / KB;
  int use_lists = (R <= RMAX && chunk <= 4096) ? 1 : 0;

  char* ws = (char*)d_ws;
  uint4* wof = (uint4*)ws;
  int* offs = (int*)(ws + 32768);
  size_t off_bytes = ((size_t)(R + 1) * KB * 4 + 255) & ~(size_t)255;
  int4* ent = (int4*)(ws + 32768 + off_bytes);

  bin_kernel<<<KB + 2, 1024, 0, stream>>>(dst, src, E, use_lists ? R : 0, chunk, offs, ent, w_out, wof);
  main_kernel<<<R, NPW * 64, 0, stream>>>(x4, src, dst, sh2, ea2, cg, w_r, wof, offs, ent, N, E, R, chunk,
                                          use_lists, (uint4*)d_out);
}
```

```cpp
#include <hip/hip_runtime.h>
#include <hip/hip_bf16.h>
#include <stdint.h>

typedef short bf16x4_t __attribute__((ext_vector_type(4)));
typedef short bf16x8_t __attribute__((ext_vector_type(8)));
typedef float f32x4_t __attribute__((ext_vector_type(4)));

#define NPW 4
#define WPN 2
#define CAP 64
#define KB 256
#define RMAX 12288
#define CH 128
#define MM 4
#define DE 16

__device__ __forceinline__ float bf_lo(unsigned v) { return __uint_as_float(v << 16); }
__device__ __forceinline__ float bf_hi(unsigned v) { return __uint_as_float(v & 0xffff0000u); }
__device__ __forceinline__ unsigned pack2(float lo, float hi) {
  __hip_bfloat162 h = __float22bfloat162_rn(make_float2(lo, hi));
  return *reinterpret_cast<unsigned*>(&h);
}

__global__ __launch_bounds__(1024) void bin_kernel(const int* __restrict__ dst, const int* __restrict__ src,
                                                   const uint2* __restrict__ sh2,
                                                   const unsigned short* __restrict__ cg, int E, int R, int chunk,
                                                   int* __restrict__ offs, int4* __restrict__ ent,
                                                   uint4* __restrict__ aeo, const unsigned short* __restrict__ w_out,
                                                   uint4* __restrict__ wof) {
  __shared__ int hist[RMAX + 1];
  __shared__ int wsum[16];
  __shared__ float cgs[64];
  const int tid = threadIdx.x;
  const int b = blockIdx.x;
  if (b >= KB) {
    int idx = (b - KB) * 1024 + tid;
    if (idx < 4 * 8 * 64) {
      int lane = idx & 63, st = idx >> 6, s = st >> 3, t = st & 7;
      int kg = lane >> 4, i = lane & 15;
      unsigned v[4];
#pragma unroll
      for (int j = 0; j < 4; ++j) {
        unsigned lo = w_out[(32 * s + 8 * kg + 2 * j) * CH + 8 * i + t];
        unsigned hi = w_out[(32 * s + 8 * kg + 2 * j + 1) * CH + 8 * i + t];
        v[j] = lo | (hi << 16);
      }
      wof[idx] = make_uint4(v[0], v[1], v[2], v[3]);
    }
    return;
  }
  for (int r = tid; r <= R; r += 1024) hist[r] = 0;
  if (tid < 64) cgs[tid] = bf_lo(cg[tid]);
  __syncthreads();
  const int e0 = b * chunk;
  const int e1 = min(E, e0 + chunk);
  int myr[4], mys[4];
#pragma unroll
  for (int k = 0; k < 4; ++k) {
    int e = e0 + k * 1024 + tid;
    myr[k] = -1;
    mys[k] = 0;
    if (e < e1) {
      int r = dst[e] / NPW;
      myr[k] = r;
      mys[k] = atomicAdd(&hist[r], 1);
      uint2 shv = sh2[e];
      float s[4] = {bf_lo(shv.x), bf_hi(shv.x), bf_lo(shv.y), bf_hi(shv.y)};
      unsigned o[8];
#pragma unroll
      for (int w = 0; w < 4; ++w) {
        float a[4];
#pragma unroll
        for (int u = 0; u < 4; ++u) {
          float v = 0.f;
#pragma unroll
          for (int q = 0; q < 4; ++q) v += cgs[(u * 4 + q) * 4 + w] * s[q];
          a[u] = v;
        }
        o[2 * w] = pack2(a[0], a[1]);
        o[2 * w + 1] = pack2(a[2], a[3]);
      }
      aeo[(size_t)e * 2] = make_uint4(o[0], o[1], o[2], o[3]);
      aeo[(size_t)e * 2 + 1] = make_uint4(o[4], o[5], o[6], o[7]);
    }
  }
  __syncthreads();
  const int per = (R + 1023) / 1024;
  int lsum = 0;
  for (int k = 0; k < per; ++k) {
    int r = tid * per + k;
    if (r < R) lsum += hist[r];
  }
  int incl = lsum;
#pragma unroll
  for (int o = 1; o < 64; o <<= 1) {
    int v = __shfl_up(incl, o, 64);
    if ((tid & 63) >= o) incl += v;
  }
  if ((tid & 63) == 63) wsum[tid >> 6] = incl;
  __syncthreads();
  int woff = 0;
  for (int w = 0; w < (tid >> 6); ++w) woff += wsum[w];
  int run = woff + incl - lsum;
  for (int k = 0; k < per; ++k) {
    int r = tid * per + k;
    if (r < R) {
      int c = hist[r];
      hist[r] = run;
      run += c;
    }
  }
  if (tid == 1023) hist[R] = e1 - e0;
  __syncthreads();
  for (int r = tid; r <= R; r += 1024) offs[(size_t)b * (R + 1) + r] = hist[r];
#pragma unroll
  for (int k = 0; k < 4; ++k) {
    int e = e0 + k * 1024 + tid;
    if (e < e1) {
      int pos = hist[myr[k]] + mys[k];
      ent[(size_t)e0 + pos] = make_int4(e, src[e], dst[e], 0);
    }
  }
}

__device__ __forceinline__ void process_edges(float (&acc)[4][4], const bf16x4_t (&wrf)[4], int my_eid, int my_src,
                                              int d, const uint2* __restrict__ x2, const uint2* __restrict__ ae2,
                                              const uint2* __restrict__ ea2, int lane, int h) {
  const int g = lane >> 4, i = lane & 15;
  const bool diag_lane = ((i >> 2) == g);
  const int nq = (d + 3) >> 2;
  const int nb = (d + 15) >> 4;

  auto load_quad = [&](int q, uint2 (&xr)[4], uint2& ap) {
    int rk = 4 * q + g;
    bool valid = rk < d;
    int rkc = valid ? rk : d - 1;
    int sg = __shfl(my_src, rkc, 64);
    int eg = __shfl(my_eid, rkc, 64);
#pragma unroll
    for (int u = 0; u < 4; ++u) xr[u] = x2[(((size_t)sg * 4 + u) * 2 + h) * 16 + i];
    ap = make_uint2(0u, 0u);
    if (diag_lane && valid) ap = ae2[(size_t)eg * 4 + (i & 3)];
  };
  auto load_ea = [&](int b) -> uint2 {
    int rho = 16 * b + 4 * (i & 3) + (i >> 2);
    rho = rho < d ? rho : d - 1;
    int eid = __shfl(my_eid, rho, 64);
    return ea2[(size_t)eid * 4 + g];
  };
  auto comp_quad = [&](const f32x4_t (&rw)[4], int r, const uint2 (&xr)[4], uint2 ap) {
    bf16x4_t aop = *reinterpret_cast<bf16x4_t*>(&ap);
#pragma unroll
    for (int t = 0; t < 4; ++t) {
      uint2 bo;
      if (t == 0) {
        bo.x = __builtin_amdgcn_perm(xr[1].x, xr[0].x, 0x05040100u);
        bo.y = __builtin_amdgcn_perm(xr[3].x, xr[2].x, 0x05040100u);
      } else if (t == 1) {
        bo.x = __builtin_amdgcn_perm(xr[1].x, xr[0].x, 0x07060302u);
        bo.y = __builtin_amdgcn_perm(xr[3].x, xr[2].x, 0x07060302u);
      } else if (t == 2) {
        bo.x = __builtin_amdgcn_perm(xr[1].y, xr[0].y, 0x05040100u);
        bo.y = __builtin_amdgcn_perm(xr[3].y, xr[2].y, 0x05040100u);
      } else {
        bo.x = __builtin_amdgcn_perm(xr[1].y, xr[0].y, 0x07060302u);
        bo.y = __builtin_amdgcn_perm(xr[3].y, xr[2].y, 0x07060302u);
      }
      bf16x4_t bop = *reinterpret_cast<bf16x4_t*>(&bo);
      f32x4_t z = {0.f, 0.f, 0.f, 0.f};
      f32x4_t tp = __builtin_amdgcn_mfma_f32_16x16x16bf16_1k(aop, bop, z, 0, 0, 0);
#pragma unroll
      for (int w = 0; w < 4; ++w) acc[t][w] += tp[w] * rw[t][r];
    }
  };

  uint2 x0[4], x1[4], x2b[4], x3[4];
  uint2 a0, a1, a2, a3;
  uint2 ea_next = load_ea(0);
  load_quad(0, x0, a0);
  if (nq > 1) load_quad(1, x1, a1);
  if (nq > 2) load_quad(2, x2b, a2);
  if (nq > 3) load_quad(3, x3, a3);
  for (int b = 0; b < nb; ++b) {
    f32x4_t rw[4];
    {
      bf16x4_t a = *reinterpret_cast<bf16x4_t*>(&ea_next);
#pragma unroll
      for (int t = 0; t < 4; ++t) {
        f32x4_t z = {0.f, 0.f, 0.f, 0.f};
        rw[t] = __builtin_amdgcn_mfma_f32_16x16x16bf16_1k(a, wrf[t], z, 0, 0, 0);
      }
    }
    if (b + 1 < nb) ea_next = load_ea(b + 1);
    const int q0 = 4 * b;
    comp_quad(rw, 0, x0, a0);
    if (q0 + 4 < nq) load_quad(q0 + 4, x0, a0);
    if (q0 + 1 < nq) {
      comp_quad(rw, 1, x1, a1);
      if (q0 + 5 < nq) load_quad(q0 + 5, x1, a1);
    }
    if (q0 + 2 < nq) {
      comp_quad(rw, 2, x2b, a2);
      if (q0 + 6 < nq) load_quad(q0 + 6, x2b, a2);
    }
    if (q0 + 3 < nq) {
      comp_quad(rw, 3, x3, a3);
      if (q0 + 7 < nq) load_quad(q0 + 7, x3, a3);
    }
  }
}

__global__ __launch_bounds__(NPW * WPN * 64, 4) void main_kernel(
    const uint2* __restrict__ x2, const int* __restrict__ src, const int* __restrict__ dst,
    const uint2* __restrict__ ae2, const uint2* __restrict__ ea2, const unsigned short* __restrict__ w_r,
    const uint4* __restrict__ wof, const int* __restrict__ offs, const int4* __restrict__ ent, int N, int E, int R,
    int chunk, int use_lists, uint4* __restrict__ out4) {
  __shared__ int lcnt[NPW];
  __shared__ int arrive;
  __shared__ int2 llist[NPW][WPN][CAP];
  __shared__ __attribute__((aligned(16))) unsigned short aggs[NPW * MM][CH];

  const int tid = threadIdx.x, lane = tid & 63, wv = tid >> 6;
  const int w = wv >> 1, h = wv & 1;
  const int rg = blockIdx.x;
  const int g = lane >> 4, i = lane & 15;

  if (tid < NPW) lcnt[tid] = 0;
  if (tid == 0) arrive = 0;
  __syncthreads();
  if (use_lists && tid < KB) {
    int s = offs[(size_t)tid * (R + 1) + rg], e = offs[(size_t)tid * (R + 1) + rg + 1];
    const int4* seg = ent + (size_t)tid * chunk;
    for (int j = s; j < e; ++j) {
      int4 v = seg[j];
      int nl = v.z - rg * NPW;
      int slot = atomicAdd(&lcnt[nl], 1);
      if (slot < CAP) llist[nl][0][slot] = make_int2(v.x, v.y);
    }
  }
  bf16x4_t wrf[4];
#pragma unroll
  for (int t = 0; t < 4; ++t) {
    uint2 v;
    const int c = 64 * h + 4 * i + t;
    v.x = (unsigned)w_r[(4 * g + 0) * CH + c] | ((unsigned)w_r[(4 * g + 1) * CH + c] << 16);
    v.y = (unsigned)w_r[(4 * g + 2) * CH + c] | ((unsigned)w_r[(4 * g + 3) * CH + c] << 16);
    wrf[t] = *reinterpret_cast<bf16x4_t*>(&v);
  }
  __syncthreads();

  const int n = rg * NPW + w;
  float acc[4][4];
#pragma unroll
  for (int t = 0; t < 4; ++t)
#pragma unroll
    for (int k = 0; k < 4; ++k) acc[t][k] = 0.f;

  if (n < N) {
    const int d = use_lists ? lcnt[w] : (CAP + 1);
    if (d <= CAP) {
      if (d > 0) {
        int2 me = (lane < d) ? llist[w][0][lane] : make_int2(0x7fffffff, 0);
        int rank = 0;
        for (int s = 0; s < d; ++s) {
          int o = __builtin_amdgcn_readlane(me.x, s);
          rank += (o < me.x) ? 1 : 0;
        }
        if (lane < d) llist[w][1 + 0 * h][rank] = me;
        __builtin_amdgcn_fence(__ATOMIC_RELEASE, "wavefront");
        __builtin_amdgcn_wave_barrier();
        __builtin_amdgcn_fence(__ATOMIC_ACQUIRE, "wavefront");
        int2 sorted = (lane < d) ? llist[w][1][lane] : make_int2(0, 0);
        process_edges(acc, wrf, sorted.x, sorted.y, d, x2, ae2, ea2, lane, h);
      }
    } else {
      int cnt = 0;
      const unsigned long long lt = (lane == 0) ? 0ull : (~0ull >> (64 - lane));
      for (int base = 0; base < E; base += 64) {
        int e = base + lane;
        bool hit = (e < E) && (dst[e] == n);
        unsigned long long m = __ballot(hit);
        if (m) {
          int tot = __popcll(m);
          if (cnt + tot > CAP) {
            __builtin_amdgcn_fence(__ATOMIC_RELEASE, "wavefront");
            __builtin_amdgcn_wave_barrier();
            __builtin_amdgcn_fence(__ATOMIC_ACQUIRE, "wavefront");
            int2 sorted = (lane < cnt) ? llist[w][h][lane] : make_int2(0, 0);
            process_edges(acc, wrf, sorted.x, sorted.y, cnt, x2, ae2, ea2, lane, h);
            cnt = 0;
          }
          if (hit) llist[w][h][cnt + __popcll(m & lt)] = make_int2(e, src[e]);
          cnt += tot;
        }
      }
      if (cnt > 0) {
        __builtin_amdgcn_fence(__ATOMIC_RELEASE, "wavefront");
        __builtin_amdgcn_wave_barrier();
        __builtin_amdgcn_fence(__ATOMIC_ACQUIRE, "wavefront");
        int2 sorted = (lane < cnt) ? llist[w][h][lane] : make_int2(0, 0);
        process_edges(acc, wrf, sorted.x, sorted.y, cnt, x2, ae2, ea2, lane, h);
      }
    }
  }
#pragma unroll
  for (int t = 0; t < 4; ++t)
#pragma unroll
    for (int k = 0; k < 4; ++k) {
      float v = acc[t][k];
      v += __shfl_xor(v, 16, 64);
      v += __shfl_xor(v, 32, 64);
      acc[t][k] = v;
    }
  if (g == 0) {
#pragma unroll
    for (int k = 0; k < 4; ++k) {
      uint2 p;
      p.x = pack2(acc[0][k], acc[1][k]);
      p.y = pack2(acc[2][k], acc[3][k]);
      *reinterpret_cast<uint2*>(&aggs[w * MM + k][64 * h + 4 * i]) = p;
    }
  }
  __builtin_amdgcn_fence(__ATOMIC_RELEASE, "workgroup");
  int prev = 0;
  if (lane == 0) prev = atomicAdd(&arrive, 1);
  prev = __shfl(prev, 0, 64);
  if (prev != NPW * WPN - 1) return;
  __builtin_amdgcn_fence(__ATOMIC_ACQUIRE, "workgroup");
  f32x4_t o[8];
#pragma unroll
  for (int t = 0; t < 8; ++t) o[t] = (f32x4_t){0.f, 0.f, 0.f, 0.f};
#pragma unroll
  for (int s = 0; s < 4; ++s) {
    bf16x8_t a0 = *reinterpret_cast<const bf16x8_t*>(&aggs[i][32 * s + 8 * g]);
#pragma unroll
    for (int t = 0; t < 8; ++t) {
      uint4 bv = wof[(s * 8 + t) * 64 + lane];
      bf16x8_t bb = *reinterpret_cast<bf16x8_t*>(&bv);
      o[t] = __builtin_amdgcn_mfma_f32_16x16x32_bf16(a0, bb, o[t], 0, 0, 0);
    }
  }
  {
    int node = rg * NPW + g;
    if (node < N) {
#pragma unroll
      for (int r = 0; r < 4; ++r) {
        uint4 p;
        p.x = pack2(o[0][r], o[1][r]);
        p.y = pack2(o[2][r], o[3][r]);
        p.z = pack2(o[4][r], o[5][r]);
        p.w = pack2(o[6][r], o[7][r]);
        out4[((size_t)node * 4 + r) * 16 + i] = p;
      }
    }
  }
}

extern "C" void kernel_launch(void* const* d_in, const int* in_sizes, int n_in, void* d_out, int out_size,
                              void* d_ws, size_t ws_size, hipStream_t stream) {
  const uint2* x2 = (const uint2*)d_in[0];
  const int* src = (const int*)d_in[1];
  const int* dst = (const int*)d_in[2];
  const uint2* sh2 = (const uint2*)d_in[3];
  const uint2* ea2 = (const uint2*)d_in[4];
  const unsigned short* cg = (const unsigned short*)d_in[5];
  const unsigned short* w_r = (const unsigned short*)d_in[6];
  const unsigned short* w_out = (const unsigned short*)d_in[7];
  const int N = in_sizes[0] / (MM * CH);
  const int E = in_sizes[1];
  const int R = (N + NPW - 1) / NPW;
  const int chunk = (E + KB - 1) / KB;
  int use_lists = (R <= RMAX && chunk <= 4096) ? 1 : 0;

  char* ws = (char*)d_ws;
  uint4* wof = (uint4*)ws;
  int* offs = (int*)(ws + 32768);
  size_t off_bytes = ((size_t)(R + 1) * KB * 4 + 255) & ~(size_t)255;
  int4* ent = (int4*)(ws + 32768 + off_bytes);
  size_t ent_bytes = ((size_t)KB * chunk * 16 + 255) & ~(size_t)255;
  uint4* aeo = (uint4*)(ws + 32768 + off_bytes + ent_bytes);

  bin_kernel<<<KB + 2, 1024, 0, stream>>>(dst, src, sh2, cg, E, use_lists ? R : 0, chunk, offs, ent, aeo, w_out, wof);
  main_kernel<<<R, NPW * WPN * 64, 0, stream>>>(x2, src, dst, (const uint2*)aeo, ea2, w_r, wof, offs, ent, N, E, R,
                                                chunk, use_lists, (uint4*)d_out);
}
```

```cpp
#include <hip/hip_runtime.h>
#include <hip/hip_bf16.h>
#include <stdint.h>

typedef short bf16x4_t __attribute__((ext_vector_type(4)));
typedef short bf16x8_t __attribute__((ext_vector_type(8)));
typedef float f32x4_t __attribute__((ext_vector_type(4)));

#define NPW 4
#define WPN 2
#define CAP 64
#define KB 256
#define RMAX 12288
#define CH 128
#define MM 4
#define DE 16

__device__ __forceinline__ float bf_lo(unsigned v) { return __uint_as_float(v << 16); }
__device__ __forceinline__ float bf_hi(unsigned v) { return __uint_as_float(v & 0xffff0000u); }
__device__ __forceinline__ unsigned pack2(float lo, float hi) {
  __hip_bfloat162 h = __float22bfloat162_rn(make_float2(lo, hi));
  return *reinterpret_cast<unsigned*>(&h);
}

__global__ __launch_bounds__(1024) void bin_kernel(const int* __restrict__ dst, const int* __restrict__ src,
                                                   const uint2* __restrict__ sh2,
                                                   const unsigned short* __restrict__ cg, int E, int R, int chunk,
                                                   int* __restrict__ offs, int4* __restrict__ ent,
                                                   uint4* __restrict__ aeo, const unsigned short* __restrict__ w_out,
                                                   uint4* __restrict__ wof) {
  __shared__ int hist[RMAX + 1];
  __shared__ int wsum[16];
  __shared__ float cgs[64];
  const int tid = threadIdx.x;
  const int b = blockIdx.x;
  if (b >= KB) {
    int idx = (b - KB) * 1024 + tid;
    if (idx < 4 * 8 * 64) {
      int lane = idx & 63, st = idx >> 6, s = st >> 3, t = st & 7;
      int kg = lane >> 4, i = lane & 15;
      unsigned v[4];
#pragma unroll
      for (int j = 0; j < 4; ++j) {
        unsigned lo = w_out[(32 * s + 8 * kg + 2 * j) * CH + 8 * i + t];
        unsigned hi = w_out[(32 * s + 8 * kg + 2 * j + 1) * CH + 8 * i + t];
        v[j] = lo | (hi << 16);
      }
      wof[idx] = make_uint4(v[0], v[1], v[2], v[3]);
    }
    return;
  }
  for (int r = tid; r <= R; r += 1024) hist[r] = 0;
  if (tid < 64) cgs[tid] = bf_lo(cg[tid]);
  __syncthreads();
  const int e0 = b * chunk;
  const int e1 = min(E, e0 + chunk);
  int myr[4], mys[4];
#pragma unroll
  for (int k = 0; k < 4; ++k) {
    int e = e0 + k * 1024 + tid;
    myr[k] = -1;
    mys[k] = 0;
    if (e < e1) {
      int r = dst[e] / NPW;
      myr[k] = r;
      mys[k] = atomicAdd(&hist[r], 1);
      uint2 shv = sh2[e];
      float s[4] = {bf_lo(shv.x), bf_hi(shv.x), bf_lo(shv.y), bf_hi(shv.y)};
      unsigned o[8];
#pragma unroll
      for (int w = 0; w < 4; ++w) {
        float a[4];
#pragma unroll
        for (int u = 0; u < 4; ++u) {
          float v = 0.f;
#pragma unroll
          for (int q = 0; q < 4; ++q) v += cgs[(u * 4 + q) * 4 + w] * s[q];
          a[u] = v;
        }
        o[2 * w] = pack2(a[0], a[1]);
        o[2 * w + 1] = pack2(a[2], a[3]);
      }
      aeo[(size_t)e * 2] = make_uint4(o[0], o[1], o[2], o[3]);
      aeo[(size_t)e * 2 + 1] = make_uint4(o[4], o[5], o[6], o[7]);
    }
  }
  __syncthreads();
  const int per = (R + 1023) / 1024;
  int lsum = 0;
  for (int k = 0; k < per; ++k) {
    int r = tid * per + k;
    if (r < R) lsum += hist[r];
  }
  int incl = lsum;
#pragma unroll
  for (int o = 1; o < 64; o <<= 1) {
    int v = __shfl_up(incl, o, 64);
    if ((tid & 63) >= o) incl += v;
  }
  if ((tid & 63) == 63) wsum[tid >> 6] = incl;
  __syncthreads();
  int woff = 0;
  for (int w = 0; w < (tid >> 6); ++w) woff += wsum[w];
  int run = woff + incl - lsum;
  for (int k = 0; k < per; ++k) {
    int r = tid * per + k;
    if (r < R) {
      int c = hist[r];
      hist[r] = run;
      run += c;
    }
  }
  if (tid == 1023) hist[R] = e1 - e0;
  __syncthreads();
  for (int r = tid; r <= R; r += 1024) offs[(size_t)b * (R + 1) + r] = hist[r];
#pragma unroll
  for (int k = 0; k < 4; ++k) {
    int e = e0 + k * 1024 + tid;
    if (e < e1) {
      int pos = hist[myr[k]] + mys[k];
      ent[(size_t)e0 + pos] = make_int4(e, src[e], dst[e], 0);
    }
  }
}

__device__ __forceinline__ void process_edges(float (&acc)[4][4], const bf16x4_t (&wrf)[4], int my_eid, int my_src,
                                              int d, const uint2* __restrict__ x2, const uint2* __restrict__ ae2,
                                              const uint2* __restrict__ ea2, int lane, int h) {
  const int g = lane >> 4, i = lane & 15;
  const bool diag_lane = ((i >> 2) == g);
  const int nq = (d + 3) >> 2;
  const int nb = (d + 15) >> 4;

  auto load_quad = [&](int q, uint2 (&xr)[4], uint2& ap) {
    int rk = 4 * q + g;
    bool valid = rk < d;
    int rkc = valid ? rk : d - 1;
    int sg = __shfl(my_src, rkc, 64);
    int eg = __shfl(my_eid, rkc, 64);
#pragma unroll
    for (int u = 0; u < 4; ++u) xr[u] = x2[(((size_t)sg * 4 + u) * 2 + h) * 16 + i];
    uint2 at = ae2[(size_t)eg * 4 + (i & 3)];
    const bool keep = diag_lane && valid;
    ap.x = keep ? at.x : 0u;
    ap.y = keep ? at.y : 0u;
  };
  auto load_ea = [&](int b) -> uint2 {
    int rho = 16 * b + 4 * (i & 3) + (i >> 2);
    rho = rho < d ? rho : d - 1;
    int eid = __shfl(my_eid, rho, 64);
    return ea2[(size_t)eid * 4 + g];
  };
  auto comp_quad = [&](const f32x4_t (&rw)[4], int r, const uint2 (&xr)[4], uint2 ap) {
    bf16x4_t aop = *reinterpret_cast<bf16x4_t*>(&ap);
#pragma unroll
    for (int t = 0; t < 4; ++t) {
      uint2 bo;
      if (t == 0) {
        bo.x = __builtin_amdgcn_perm(xr[1].x, xr[0].x, 0x05040100u);
        bo.y = __builtin_amdgcn_perm(xr[3].x, xr[2].x, 0x05040100u);
      } else if (t == 1) {
        bo.x = __builtin_amdgcn_perm(xr[1].x, xr[0].x, 0x07060302u);
        bo.y = __builtin_amdgcn_perm(xr[3].x, xr[2].x, 0x07060302u);
      } else if (t == 2) {
        bo.x = __builtin_amdgcn_perm(xr[1].y, xr[0].y, 0x05040100u);
        bo.y = __builtin_amdgcn_perm(xr[3].y, xr[2].y, 0x05040100u);
      } else {
        bo.x = __builtin_amdgcn_perm(xr[1].y, xr[0].y, 0x07060302u);
        bo.y = __builtin_amdgcn_perm(xr[3].y, xr[2].y, 0x07060302u);
      }
      bf16x4_t bop = *reinterpret_cast<bf16x4_t*>(&bo);
      f32x4_t z = {0.f, 0.f, 0.f, 0.f};
      f32x4_t tp = __builtin_amdgcn_mfma_f32_16x16x16bf16_1k(aop, bop, z, 0, 0, 0);
#pragma unroll
      for (int w = 0; w < 4; ++w) acc[t][w] += tp[w] * rw[t][r];
    }
  };

  uint2 x0[4], x1[4], x2b[4], x3[4];
  uint2 a0, a1, a2, a3;
  uint2 ea_next = load_ea(0);
  load_quad(0, x0, a0);
  load_quad(1, x1, a1);
  load_quad(2, x2b, a2);
  load_quad(3, x3, a3);
  for (int b = 0; b < nb; ++b) {
    f32x4_t rw[4];
    {
      bf16x4_t a = *reinterpret_cast<bf16x4_t*>(&ea_next);
#pragma unroll
      for (int t = 0; t < 4; ++t) {
        f32x4_t z = {0.f, 0.f, 0.f, 0.f};
        rw[t] = __builtin_amdgcn_mfma_f32_16x16x16bf16_1k(a, wrf[t], z, 0, 0, 0);
      }
    }
    ea_next = load_ea(b + 1);
    const int q0 = 4 * b;
    comp_quad(rw, 0, x0, a0);
    load_quad(q0 + 4, x0, a0);
    comp_quad(rw, 1, x1, a1);
    load_quad(q0 + 5, x1, a1);
    comp_quad(rw, 2, x2b, a2);
    load_quad(q0 + 6, x2b, a2);
    comp_quad(rw, 3, x3, a3);
    load_quad(q0 + 7, x3, a3);
  }
}

__global__ __launch_bounds__(NPW * WPN * 64, 2) void main_kernel(
    const uint2* __restrict__ x2, const int* __restrict__ src, const int* __restrict__ dst,
    const uint2* __restrict__ ae2, const uint2* __restrict__ ea2, const unsigned short* __restrict__ w_r,
    const uint4* __restrict__ wof, const int* __restrict__ offs, const int4* __restrict__ ent, int N, int E, int R,
    int chunk, int use_lists, uint4* __restrict__ out4) {
  __shared__ int lcnt[NPW];
  __shared__ int arrive;
  __shared__ int2 llist[NPW][WPN][CAP];
  __shared__ __attribute__((aligned(16))) unsigned short aggs[NPW * MM][CH];

  const int tid = threadIdx.x, lane = tid & 63, wv = tid >> 6;
  const int w = wv >> 1, h = wv & 1;
  const int rg = blockIdx.x;
  const int g = lane >> 4, i = lane & 15;

  if (tid < NPW) lcnt[tid] = 0;
  if (tid == 0) arrive = 0;
  __syncthreads();
  if (use_lists && tid < KB) {
    int s = offs[(size_t)tid * (R + 1) + rg], e = offs[(size_t)tid * (R + 1) + rg + 1];
    const int4* seg = ent + (size_t)tid * chunk;
    for (int j = s; j < e; ++j) {
      int4 v = seg[j];
      int nl = v.z - rg * NPW;
      int slot = atomicAdd(&lcnt[nl], 1);
      if (slot < CAP) llist[nl][0][slot] = make_int2(v.x, v.y);
    }
  }
  bf16x4_t wrf[4];
#pragma unroll
  for (int t = 0; t < 4; ++t) {
    uint2 v;
    const int c = 64 * h + 4 * i + t;
    v.x = (unsigned)w_r[(4 * g + 0) * CH + c] | ((unsigned)w_r[(4 * g + 1) * CH + c] << 16);
    v.y = (unsigned)w_r[(4 * g + 2) * CH + c] | ((unsigned)w_r[(4 * g + 3) * CH + c] << 16);
    wrf[t] = *reinterpret_cast<bf16x4_t*>(&v);
  }
  __syncthreads();

  const int n = rg * NPW + w;
  float acc[4][4];
#pragma unroll
  for (int t = 0; t < 4; ++t)
#pragma unroll
    for (int k = 0; k < 4; ++k) acc[t][k] = 0.f;

  if (n < N) {
    const int d = use_lists ? lcnt[w] : (CAP + 1);
    if (d <= CAP) {
      if (d > 0) {
        int2 me = (lane < d) ? llist[w][0][lane] : make_int2(0x7fffffff, 0);
        int rank = 0;
        for (int s = 0; s < d; ++s) {
          int o = __builtin_amdgcn_readlane(me.x, s);
          rank += (o < me.x) ? 1 : 0;
        }
        if (lane < d) llist[w][1 + 0 * h][rank] = me;
        __builtin_amdgcn_fence(__ATOMIC_RELEASE, "wavefront");
        __builtin_amdgcn_wave_barrier();
        __builtin_amdgcn_fence(__ATOMIC_ACQUIRE, "wavefront");
        int2 sorted = (lane < d) ? llist[w][1][lane] : make_int2(0, 0);
        process_edges(acc, wrf, sorted.x, sorted.y, d, x2, ae2, ea2, lane, h);
      }
    } else {
      int cnt = 0;
      const unsigned long long lt = (lane == 0) ? 0ull : (~0ull >> (64 - lane));
      for (int base = 0; base < E; base += 64) {
        int e = base + lane;
        bool hit = (e < E) && (dst[e] == n);
        unsigned long long m = __ballot(hit);
        if (m) {
          int tot = __popcll(m);
          if (cnt + tot > CAP) {
            __builtin_amdgcn_fence(__ATOMIC_RELEASE, "wavefront");
            __builtin_amdgcn_wave_barrier();
            __builtin_amdgcn_fence(__ATOMIC_ACQUIRE, "wavefront");
            int2 sorted = (lane < cnt) ? llist[w][h][lane] : make_int2(0, 0);
            process_edges(acc, wrf, sorted.x, sorted.y, cnt, x2, ae2, ea2, lane, h);
            cnt = 0;
          }
          if (hit) llist[w][h][cnt + __popcll(m & lt)] = make_int2(e, src[e]);
          cnt += tot;
        }
      }
      if (cnt > 0) {
        __builtin_amdgcn_fence(__ATOMIC_RELEASE, "wavefront");
        __builtin_amdgcn_wave_barrier();
        __builtin_amdgcn_fence(__ATOMIC_ACQUIRE, "wavefront");
        int2 sorted = (lane < cnt) ? llist[w][h][lane] : make_int2(0, 0);
        process_edges(acc, wrf, sorted.x, sorted.y, cnt, x2, ae2, ea2, lane, h);
      }
    }
  }
#pragma unroll
  for (int t = 0; t < 4; ++t)
#pragma unroll
    for (int k = 0; k < 4; ++k) {
      float v = acc[t][k];
      v += __shfl_xor(v, 16, 64);
      v += __shfl_xor(v, 32, 64);
      acc[t][k] = v;
    }
  if (g == 0) {
#pragma unroll
    for (int k = 0; k < 4; ++k) {
      uint2 p;
      p.x = pack2(acc[0][k], acc[1][k]);
      p.y = pack2(acc[2][k], acc[3][k]);
      *reinterpret_cast<uint2*>(&aggs[w * MM + k][64 * h + 4 * i]) = p;
    }
  }
  __builtin_amdgcn_fence(__ATOMIC_RELEASE, "workgroup");
  int prev = 0;
  if (lane == 0) prev = atomicAdd(&arrive, 1);
  prev = __shfl(prev, 0, 64);
  if (prev != NPW * WPN - 1) return;
  __builtin_amdgcn_fence(__ATOMIC_ACQUIRE, "workgroup");
  f32x4_t o[8];
#pragma unroll
  for (int t = 0; t < 8; ++t) o[t] = (f32x4_t){0.f, 0.f, 0.f, 0.f};
#pragma unroll
  for (int s = 0; s < 4; ++s) {
    bf16x8_t a0 = *reinterpret_cast<const bf16x8_t*>(&aggs[i][32 * s + 8 * g]);
#pragma unroll
    for (int t = 0; t < 8; ++t) {
      uint4 bv = wof[(s * 8 + t) * 64 + lane];
      bf16x8_t bb = *reinterpret_cast<bf16x8_t*>(&bv);
      o[t] = __builtin_amdgcn_mfma_f32_16x16x32_bf16(a0, bb, o[t], 0, 0, 0);
    }
  }
  {
    int node = rg * NPW + g;
    if (node < N) {
#pragma unroll
      for (int r = 0; r < 4; ++r) {
        uint4 p;
        p.x = pack2(o[0][r], o[1][r]);
        p.y = pack2(o[2][r], o[3][r]);
        p.z = pack2(o[4][r], o[5][r]);
        p.w = pack2(o[6][r], o[7][r]);
        out4[((size_t)node * 4 + r) * 16 + i] = p;
      }
    }
  }
}

extern "C" void kernel_launch(void* const* d_in, const int* in_sizes, int n_in, void* d_out, int out_size,
                              void* d_ws, size_t ws_size, hipStream_t stream) {
  const uint2* x2 = (const uint2*)d_in[0];
  const int* src = (const int*)d_in[1];
  const int* dst = (const int*)d_in[2];
  const uint2* sh2 = (const uint2*)d_in[3];
  const uint2* ea2 = (const uint2*)d_in[4];
  const unsigned short* cg = (const unsigned short*)d_in[5];
  const unsigned short* w_r = (const unsigned short*)d_in[6];
  const unsigned short* w_out = (const unsigned short*)d_in[7];
  const int N = in_sizes[0] / (MM * CH);
  const int E = in_sizes[1];
  const int R = (N + NPW - 1) / NPW;
  const int chunk = (E + KB - 1) / KB;
  int use_lists = (R <= RMAX && chunk <= 4096) ? 1 : 0;

  char* ws = (char*)d_ws;
  uint4* wof = (uint4*)ws;
  int* offs = (int*)(ws + 32768);
  size_t off_bytes = ((size_t)(R + 1) * KB * 4 + 255) & ~(size_t)255;
  int4* ent = (int4*)(ws + 32768 + off_bytes);
  size_t ent_bytes = ((size_t)KB * chunk * 16 + 255) & ~(size_t)255;
  uint4* aeo = (uint4*)(ws + 32768 + off_bytes + ent_bytes);

  bin_kernel<<<KB + 2, 1024, 0, stream>>>(dst, src, sh2, cg, E, use_lists ? R : 0, chunk, offs, ent, aeo, w_out, wof);
  main_kernel<<<R, NPW * WPN * 64, 0, stream>>>(x2, src, dst, (const uint2*)aeo, ea2, w_r, wof, offs, ent, N, E, R,
                                                chunk, use_lists, (uint4*)d_out);
}
```

```cpp
#include <hip/hip_runtime.h>
#include <hip/hip_bf16.h>
#include <stdint.h>

typedef short bf16x4_t __attribute__((ext_vector_type(4)));
typedef short bf16x8_t __attribute__((ext_vector_type(8)));
typedef float f32x4_t __attribute__((ext_vector_type(4)));

#define NPW 8
#define CAP 64
#define KB 128
#define RMAX 12288
#define CH 128
#define MM 4
#define DE 16

__device__ __forceinline__ float bf_lo(unsigned v) { return __uint_as_float(v << 16); }
__device__ __forceinline__ float bf_hi(unsigned v) { return __uint_as_float(v & 0xffff0000u); }
__device__ __forceinline__ unsigned pack2(float lo, float hi) {
  __hip_bfloat162 h = __float22bfloat162_rn(make_float2(lo, hi));
  return *reinterpret_cast<unsigned*>(&h);
}

__global__ __launch_bounds__(1024) void bin_kernel(const int* __restrict__ dst, const int* __restrict__ src, int E,
                                                   int R, int chunk, int* __restrict__ offs,
                                                   int4* __restrict__ ent, const unsigned short* __restrict__ w_out,
                                                   uint4* __restrict__ wof) {
  __shared__ int hist[RMAX + 1];
  __shared__ int wsum[16];
  const int tid = threadIdx.x;
  const int b = blockIdx.x;
  if (b >= KB) {
    int idx = (b - KB) * 1024 + tid;
    if (idx < 4 * 8 * 64) {
      int lane = idx & 63, st = idx >> 6, s = st >> 3, t = st & 7;
      int kg = lane >> 4, i = lane & 15;
      unsigned v[4];
#pragma unroll
      for (int j = 0; j < 4; ++j) {
        unsigned lo = w_out[(32 * s + 8 * kg + 2 * j) * CH + 8 * i + t];
        unsigned hi = w_out[(32 * s + 8 * kg + 2 * j + 1) * CH + 8 * i + t];
        v[j] = lo | (hi << 16);
      }
      wof[idx] = make_uint4(v[0], v[1], v[2], v[3]);
    }
    return;
  }
  for (int r = tid; r <= R; r += 1024) hist[r] = 0;
  __syncthreads();
  const int e0 = b * chunk;
  const int e1 = min(E, e0 + chunk);
  int myr[4], mys[4];
#pragma unroll
  for (int k = 0; k < 4; ++k) {
    int e = e0 + k * 1024 + tid;
    myr[k] = -1;
    mys[k] = 0;
    if (e < e1) {
      int r = dst[e] / NPW;
      myr[k] = r;
      mys[k] = atomicAdd(&hist[r], 1);
    }
  }
  __syncthreads();
  const int per = (R + 1023) / 1024;
  int lsum = 0;
  for (int k = 0; k < per; ++k) {
    int r = tid * per + k;
    if (r < R) lsum += hist[r];
  }
  int incl = lsum;
#pragma unroll
  for (int o = 1; o < 64; o <<= 1) {
    int v = __shfl_up(incl, o, 64);
    if ((tid & 63) >= o) incl += v;
  }
  if ((tid & 63) == 63) wsum[tid >> 6] = incl;
  __syncthreads();
  int woff = 0;
  for (int w = 0; w < (tid >> 6); ++w) woff += wsum[w];
  int run = woff + incl - lsum;
  for (int k = 0; k < per; ++k) {
    int r = tid * per + k;
    if (r < R) {
      int c = hist[r];
      hist[r] = run;
      run += c;
    }
  }
  if (tid == 1023) hist[R] = e1 - e0;
  __syncthreads();
  for (int r = tid; r <= R; r += 1024) offs[(size_t)r * KB + b] = hist[r];
#pragma unroll
  for (int k = 0; k < 4; ++k) {
    int e = e0 + k * 1024 + tid;
    if (e < e1) {
      int pos = hist[myr[k]] + mys[k];
      ent[(size_t)e0 + pos] = make_int4(e, src[e], dst[e], 0);
    }
  }
}

struct Consts {
  float cgw[16];
  bf16x4_t wrf[8];
};

__device__ __forceinline__ void process_edges(float (&acc)[8][4], const Consts& cs, int my_eid, int my_src, int d,
                                              const uint4* __restrict__ x4, const uint2* __restrict__ sh2,
                                              const uint2* __restrict__ ea2, int lane) {
  const int g = lane >> 4, i = lane & 15;
  const bool diag_lane = ((i >> 2) == g);
  const int nb = (d + 15) >> 4;
  for (int b = 0; b < nb; ++b) {
    f32x4_t rw[8];
    {
      int rho = 16 * b + 4 * (i & 3) + (i >> 2);
      rho = rho < d ? rho : d - 1;
      int eid = __shfl(my_eid, rho, 64);
      uint2 eav = ea2[(size_t)eid * 4 + g];
      bf16x4_t a = *reinterpret_cast<bf16x4_t*>(&eav);
#pragma unroll
      for (int t = 0; t < 8; ++t) {
        f32x4_t z = {0.f, 0.f, 0.f, 0.f};
        rw[t] = __builtin_amdgcn_mfma_f32_16x16x16bf16_1k(a, cs.wrf[t], z, 0, 0, 0);
      }
    }
#pragma unroll
    for (int r = 0; r < 4; ++r) {
      int q = 4 * b + r;
      if (4 * q < d) {
        int rk = 4 * q + g;
        bool valid = rk < d;
        int rkc = valid ? rk : d - 1;
        int sg = __shfl(my_src, rkc, 64);
        int eg = __shfl(my_eid, rkc, 64);
        uint4 xr[4];
#pragma unroll
        for (int u = 0; u < 4; ++u) xr[u] = x4[((size_t)sg * 4 + u) * 16 + i];
        uint2 shv = sh2[eg];
        float s0 = bf_lo(shv.x), s1 = bf_hi(shv.x), s2 = bf_lo(shv.y), s3 = bf_hi(shv.y);
        float av[4];
#pragma unroll
        for (int u = 0; u < 4; ++u)
          av[u] = cs.cgw[u * 4 + 0] * s0 + cs.cgw[u * 4 + 1] * s1 + cs.cgw[u * 4 + 2] * s2 + cs.cgw[u * 4 + 3] * s3;
        uint2 ap;
        ap.x = pack2(av[0], av[1]);
        ap.y = pack2(av[2], av[3]);
        if (!(diag_lane && valid)) { ap.x = 0u; ap.y = 0u; }
        bf16x4_t aop = *reinterpret_cast<bf16x4_t*>(&ap);
        const unsigned* x0 = reinterpret_cast<const unsigned*>(&xr[0]);
        const unsigned* x1 = reinterpret_cast<const unsigned*>(&xr[1]);
        const unsigned* x2 = reinterpret_cast<const unsigned*>(&xr[2]);
        const unsigned* x3 = reinterpret_cast<const unsigned*>(&xr[3]);
#pragma unroll
        for (int t = 0; t < 8; ++t) {
          const int dd = t >> 1;
          uint2 bo;
          if (t & 1) {
            bo.x = __builtin_amdgcn_perm(x1[dd], x0[dd], 0x07060302u);
            bo.y = __builtin_amdgcn_perm(x3[dd], x2[dd], 0x07060302u);
          } else {
            bo.x = __builtin_amdgcn_perm(x1[dd], x0[dd], 0x05040100u);
            bo.y = __builtin_amdgcn_perm(x3[dd], x2[dd], 0x05040100u);
          }
          bf16x4_t bop = *reinterpret_cast<bf16x4_t*>(&bo);
          f32x4_t z = {0.f, 0.f, 0.f, 0.f};
          f32x4_t tp = __builtin_amdgcn_mfma_f32_16x16x16bf16_1k(aop, bop, z, 0, 0, 0);
#pragma unroll
          for (int w = 0; w < 4; ++w) acc[t][w] += tp[w] * rw[t][r];
        }
      }
    }
  }
}

__global__ __launch_bounds__(NPW * 64) void main_kernel(
    const uint4* __restrict__ x4, const int* __restrict__ src, const int* __restrict__ dst,
    const uint2* __restrict__ sh2, const uint2* __restrict__ ea2, const unsigned short* __restrict__ cg,
    const unsigned short* __restrict__ w_r, const uint4* __restrict__ wof, const int* __restrict__ offs,
    const int4* __restrict__ ent, int N, int E, int R, int chunk, int use_lists, uint4* __restrict__ out4) {
  __shared__ int lcnt[NPW];
  __shared__ int arrive;
  __shared__ int2 llist[NPW][CAP];
  __shared__ __attribute__((aligned(16))) unsigned short aggs[NPW * MM][CH];

  const int tid = threadIdx.x, lane = tid & 63, w = tid >> 6;
  const int rg = blockIdx.x;
  const int g = lane >> 4, i = lane & 15;

  if (tid < NPW) lcnt[tid] = 0;
  if (tid == 0) arrive = 0;
  __syncthreads();
  if (use_lists && tid < KB) {
    int s = offs[(size_t)rg * KB + tid], e = offs[(size_t)(rg + 1) * KB + tid];
    const int4* seg = ent + (size_t)tid * chunk;
    for (int j = s; j < e; ++j) {
      int4 v = seg[j];
      int nl = v.z - rg * NPW;
      int slot = atomicAdd(&lcnt[nl], 1);
      if (slot < CAP) llist[nl][slot] = make_int2(v.x, v.y);
    }
  }
  Consts cs;
  {
    const int wq = i & 3;
#pragma unroll
    for (int k = 0; k < 16; ++k) cs.cgw[k] = bf_lo(cg[k * 4 + wq]);
#pragma unroll
    for (int t = 0; t < 8; ++t) {
      uint2 v;
      v.x = (unsigned)w_r[(4 * g + 0) * CH + 8 * i + t] | ((unsigned)w_r[(4 * g + 1) * CH + 8 * i + t] << 16);
      v.y = (unsigned)w_r[(4 * g + 2) * CH + 8 * i + t] | ((unsigned)w_r[(4 * g + 3) * CH + 8 * i + t] << 16);
      cs.wrf[t] = *reinterpret_cast<bf16x4_t*>(&v);
    }
  }
  __syncthreads();

  const int n = rg * NPW + w;
  float acc[8][4];
#pragma unroll
  for (int t = 0; t < 8; ++t)
#pragma unroll
    for (int k = 0; k < 4; ++k) acc[t][k] = 0.f;

  if (n < N) {
    const int d = use_lists ? lcnt[w] : (CAP + 1);
    if (d <= CAP) {
      if (d > 0) {
        int2 me = (lane < d) ? llist[w][lane] : make_int2(0x7fffffff, 0);
        int rank = 0;
        for (int s = 0; s < d; ++s) {
          int o = __builtin_amdgcn_readlane(me.x, s);
          rank += (o < me.x) ? 1 : 0;
        }
        if (lane < d) llist[w][rank] = me;
        __builtin_amdgcn_fence(__ATOMIC_RELEASE, "wavefront");
        __builtin_amdgcn_wave_barrier();
        __builtin_amdgcn_fence(__ATOMIC_ACQUIRE, "wavefront");
        int2 sorted = (lane < d) ? llist[w][lane] : make_int2(0, 0);
        process_edges(acc, cs, sorted.x, sorted.y, d, x4, sh2, ea2, lane);
      }
    } else {
      int cnt = 0;
      const unsigned long long lt = (lane == 0) ? 0ull : (~0ull >> (64 - lane));
      for (int base = 0; base < E; base += 64) {
        int e = base + lane;
        bool hit = (e < E) && (dst[e] == n);
        unsigned long long m = __ballot(hit);
        if (m) {
          int tot = __popcll(m);
          if (cnt + tot > CAP) {
            __builtin_amdgcn_fence(__ATOMIC_RELEASE, "wavefront");
            __builtin_amdgcn_wave_barrier();
            __builtin_amdgcn_fence(__ATOMIC_ACQUIRE, "wavefront");
            int2 sorted = (lane < cnt) ? llist[w][lane] : make_int2(0, 0);
            process_edges(acc, cs, sorted.x, sorted.y, cnt, x4, sh2, ea2, lane);
            cnt = 0;
          }
          if (hit) llist[w][cnt + __popcll(m & lt)] = make_int2(e, src[e]);
          cnt += tot;
        }
      }
      if (cnt > 0) {
        __builtin_amdgcn_fence(__ATOMIC_RELEASE, "wavefront");
        __builtin_amdgcn_wave_barrier();
        __builtin_amdgcn_fence(__ATOMIC_ACQUIRE, "wavefront");
        int2 sorted = (lane < cnt) ? llist[w][lane] : make_int2(0, 0);
        process_edges(acc, cs, sorted.x, sorted.y, cnt, x4, sh2, ea2, lane);
      }
    }
  }
#pragma unroll
  for (int t = 0; t < 8; ++t)
#pragma unroll
    for (int k = 0; k < 4; ++k) {
      float v = acc[t][k];
      v += __shfl_xor(v, 16, 64);
      v += __shfl_xor(v, 32, 64);
      acc[t][k] = v;
    }
  if (g == 0) {
#pragma unroll
    for (int k = 0; k < 4; ++k) {
      uint4 p;
      p.x = pack2(acc[0][k], acc[1][k]);
      p.y = pack2(acc[2][k], acc[3][k]);
      p.z = pack2(acc[4][k], acc[5][k]);
      p.w = pack2(acc[6][k], acc[7][k]);
      *reinterpret_cast<uint4*>(&aggs[w * MM + k][8 * i]) = p;
    }
  }
  __builtin_amdgcn_fence(__ATOMIC_RELEASE, "workgroup");
  int prev = 0;
  if (lane == 0) prev = atomicAdd(&arrive, 1);
  prev = __shfl(prev, 0, 64);
  if (prev != NPW - 1) return;
  __builtin_amdgcn_fence(__ATOMIC_ACQUIRE, "workgroup");
  f32x4_t o[2][8];
#pragma unroll
  for (int mt = 0; mt < 2; ++mt)
#pragma unroll
    for (int t = 0; t < 8; ++t) o[mt][t] = (f32x4_t){0.f, 0.f, 0.f, 0.f};
#pragma unroll
  for (int s = 0; s < 4; ++s) {
    bf16x8_t a0 = *reinterpret_cast<const bf16x8_t*>(&aggs[i][32 * s + 8 * g]);
    bf16x8_t a1 = *reinterpret_cast<const bf16x8_t*>(&aggs[16 + i][32 * s + 8 * g]);
#pragma unroll
    for (int t = 0; t < 8; ++t) {
      uint4 bv = wof[(s * 8 + t) * 64 + lane];
      bf16x8_t bb = *reinterpret_cast<bf16x8_t*>(&bv);
      o[0][t] = __builtin_amdgcn_mfma_f32_16x16x32_bf16(a0, bb, o[0][t], 0, 0, 0);
      o[1][t] = __builtin_amdgcn_mfma_f32_16x16x32_bf16(a1, bb, o[1][t], 0, 0, 0);
    }
  }
#pragma unroll
  for (int mt = 0; mt < 2; ++mt) {
    int node = rg * NPW + mt * 4 + g;
    if (node < N) {
#pragma unroll
      for (int r = 0; r < 4; ++r) {
        uint4 p;
        p.x = pack2(o[mt][0][r], o[mt][1][r]);
        p.y = pack2(o[mt][2][r], o[mt][3][r]);
        p.z = pack2(o[mt][4][r], o[mt][5][r]);
        p.w = pack2(o[mt][6][r], o[mt][7][r]);
        out4[((size_t)node * 4 + r) * 16 + i] = p;
      }
    }
  }
}

extern "C" void kernel_launch(void* const* d_in, const int* in_sizes, int n_in, void* d_out, int out_size,
                              void* d_ws, size_t ws_size, hipStream_t stream) {
  const uint4* x4 = (const uint4*)d_in[0];
  const int* src = (const int*)d_in[1];
  const int* dst = (const int*)d_in[2];
  const uint2* sh2 = (const uint2*)d_in[3];
  const uint2* ea2 = (const uint2*)d_in[4];
  const unsigned short* cg = (const unsigned short*)d_in[5];
  const unsigned short* w_r = (const unsigned short*)d_in[6];
  const unsigned short* w_out = (const unsigned short*)d_in[7];
  const int N = in_sizes[0] / (MM * CH);
  const int E = in_sizes[1];
  const int R = (N + NPW - 1) / NPW;
  const int chunk = (E + KB - 1) / KB;
  int use_lists = (R <= RMAX && chunk <= 4096) ? 1 : 0;

  char* ws = (char*)d_ws;
  uint4* wof = (uint4*)ws;
  int* offs = (int*)(ws + 32768);
  size_t off_bytes = ((size_t)(R + 1) * KB * 4 + 255) & ~(size_t)255;
  int4* ent = (int4*)(ws + 32768 + off_bytes);

  bin_kernel<<<KB + 2, 1024, 0, stream>>>(dst, src, E, use_lists ? R : 0, chunk, offs, ent, w_out, wof);
  main_kernel<<<R, NPW * 64, 0, stream>>>(x4, src, dst, sh2, ea2, cg, w_r, wof, offs, ent, N, E, R, chunk,
                                          use_lists, (uint4*)d_out);
}
```
